```python
import torch

def _softmax(x, axis=-1):
    return torch.softmax(x, dim=axis)
def _gelu(x):
    return torch.nn.functional.gelu(x, approximate="tanh")
def _silu(x):
    return torch.nn.functional.silu(x)
def _sigmoid(x):
    return torch.sigmoid(x)
def _layernorm(x, axis=-1, eps=1e-5):
    m = x.mean(dim=axis, keepdim=True)
    v = x.var(dim=axis, keepdim=True, unbiased=False)
    return (x - m) / torch.sqrt(v + eps)
def _scatter_add(tgt, idx, src, axis=0):
    return tgt.clone().index_add_(axis, idx, src)
def _segment_sum(src, seg, n):
    out = torch.zeros((n,) + src.shape[1:], dtype=src.dtype)
    return out.index_add_(0, seg, src)
def _pad(x, pw):
    flat = tuple(v for pair in reversed(pw) for v in pair)
    return torch.nn.functional.pad(x, flat)

kernel_name = 'recurrent_scan__mha_block__moe_arch_mod__prompt'

def setup_inputs():

    _w = (lambda a: (lambda t: t.to(torch.bfloat16) if t.is_floating_point() else t)(torch.as_tensor(a)))
    def _A_setup():
        B, S, D, H = 1, 2048, 448, 16
        return {
            "x": _w((lambda *s: torch.randn(*s, dtype=torch.bfloat16) * 0.05)(B, S, D)),
            "wqkv": _w((lambda *s: torch.randn(*s, dtype=torch.bfloat16) * 0.05)(D, 3 * D)),
            "wo": _w((lambda *s: torch.randn(*s, dtype=torch.bfloat16) * 0.05)(D, D)),
        }
    _A_in = _A_setup()

    def _B_setup():
        B, S, D, F, C = 1, 2048, 448, 1792, 1
        return {
            "x": _w((lambda *s: torch.randn(*s, dtype=torch.bfloat16) * 0.05)(B, S, D)),
            "w_r": _w((lambda *s: torch.randn(*s, dtype=torch.bfloat16) * 0.05)(D)),
            "w_gate": _w((lambda *s: torch.randn(*s, dtype=torch.bfloat16) * 0.05)(D, F)),
            "w_up": _w((lambda *s: torch.randn(*s, dtype=torch.bfloat16) * 0.05)(D, F)),
            "w_down": _w((lambda *s: torch.randn(*s, dtype=torch.bfloat16) * 0.05)(F, D)),
        }
    _B_in = _B_setup()

    return {
        "A_x": _A_in["x"],
        "A_wqkv": _A_in["wqkv"],
        "A_wo": _A_in["wo"],
        "B_x": _B_in["x"],
        "B_w_r": _B_in["w_r"],
        "B_w_gate": _B_in["w_gate"],
        "B_w_up": _B_in["w_up"],
        "B_w_down": _B_in["w_down"],
    }

def reference(A_x, A_wqkv, A_wo, B_x, B_w_r, B_w_gate, B_w_up, B_w_down):
    def _A_ref(x, wqkv, wo):
        B, S, D = x.shape
        H = 16
        Dh = D // H
        qkv = (lambda x, *d: x.permute(*d))((x @ wqkv).reshape(B, S, 3, H, Dh), 2, 0, 3, 1, 4)
        q, k, v = qkv[0], qkv[1], qkv[2]
        scores = torch.einsum("bhqd,bhkd->bhqk", q, k) * (Dh ** -0.5)
        p = _softmax(scores, axis=-1)
        o = torch.einsum("bhqk,bhkd->bhqd", p, v)
        return (lambda x, *d: x.permute(*d))(o, 0, 2, 1, 3).reshape(B, S, D) @ wo

    def _B_ref(x, w_r, w_gate, w_up, w_down):
        B, S, D = x.shape
        C = 1
        r = x @ w_r
        idx = torch.argsort(-r, axis=-1)[:, :C]
        idx_e = (lambda x, n, axis: torch.repeat_interleave(x, n, dim=axis))(idx.reshape(B, C, 1), D, axis=2)
        x_sel = (lambda x, i, axis: torch.take_along_dim(x, i, dim=axis))(x, idx_e, axis=1)
        g = _silu(x_sel @ w_gate)
        u = x_sel @ w_up
        y = (g * u) @ w_down
        r_sel = _sigmoid((lambda x, i, axis: torch.take_along_dim(x, i, dim=axis))(r, idx, axis=1))
        y = y * r_sel.reshape(B, C, 1)
        b_off = (torch.arange(B) * S).reshape(B, 1)
        flat_idx = (idx + b_off).reshape(B * C)
        zero = (x * 0.0).reshape(B * S, D)
        upd = _scatter_add(zero, flat_idx, y.reshape(B * C, D), axis=0)
        return x + upd.reshape(B, S, D)

    x = A_x
    xn = _layernorm(x, axis=-1)
    s = _A_ref(xn, A_wqkv, A_wo)
    g = _silu(_B_ref(xn, B_w_r, B_w_gate, B_w_up, B_w_down))
    return x + s * g
```

```cpp
#include <hip/hip_runtime.h>
#include <hip/hip_bf16.h>
#include <stdint.h>

typedef short bf16x8 __attribute__((ext_vector_type(8)));
typedef short s16x4 __attribute__((ext_vector_type(4)));
typedef float f32x16 __attribute__((ext_vector_type(16)));
typedef float f32x4 __attribute__((ext_vector_type(4)));
typedef unsigned u32x4 __attribute__((ext_vector_type(4)));
typedef unsigned u32x2 __attribute__((ext_vector_type(2)));
typedef __bf16 bf2 __attribute__((ext_vector_type(2)));
typedef unsigned short u16;
#define LDSAS __attribute__((address_space(3)))

namespace {

constexpr int SEQ = 2048, DM = 448, NH = 16, DH = 28, FF = 1792, N3 = 1344;
constexpr float LN_EPS = 1e-5f;
constexpr float QSCALE = 0.18898223650461362f * 1.4426950408889634f;
constexpr unsigned ONES_BF2 = 0x3F803F80u;

constexpr size_t TILE_B = 2048;
constexpr size_t OFF_Q = 0;
constexpr size_t OFF_K = OFF_Q + (size_t)NH * 64 * TILE_B;
constexpr size_t OFF_V = OFF_K + (size_t)NH * 64 * TILE_B;
constexpr size_t OFF_O = OFF_V + (size_t)NH * 64 * TILE_B;
constexpr size_t OFF_R = OFF_O + (size_t)64 * 56 * 512;
constexpr size_t OFF_ST = OFF_R + 2048 * 4;
constexpr size_t OFF_MP = OFF_ST + 2048 * 8;
constexpr int MOE_G = 56;
constexpr int MOE_FS = FF / MOE_G;
constexpr size_t OFF_MI = OFF_MP + (size_t)MOE_G * 448 * 4;

__device__ __forceinline__ unsigned pack_bf16(float lo, float hi) {
  bf2 v = {(__bf16)lo, (__bf16)hi};
  return __builtin_bit_cast(unsigned, v);
}
__device__ __forceinline__ float bf_lo(unsigned u) { return __uint_as_float(u << 16); }
__device__ __forceinline__ float bf_hi(unsigned u) { return __uint_as_float(u & 0xffff0000u); }
__device__ __forceinline__ float dot2bf(unsigned a, unsigned b, float c) {
  return __builtin_amdgcn_fdot2_f32_bf16(__builtin_bit_cast(bf2, a), __builtin_bit_cast(bf2, b), c, false);
}
__device__ __forceinline__ float bfround(float x) {
  return (float)((__bf16)x);
}
#define MFMA32(a, b, c) __builtin_amdgcn_mfma_f32_32x32x16_bf16((a), (b), (c), 0, 0, 0)

__device__ __forceinline__ bf16x8 tr_frag(const char* p) {
  s16x4 lo = __builtin_amdgcn_ds_read_tr16_b64_v4i16((LDSAS s16x4*)(p));
  s16x4 hi = __builtin_amdgcn_ds_read_tr16_b64_v4i16((LDSAS s16x4*)(p + 256));
  return __builtin_shufflevector(lo, hi, 0, 1, 2, 3, 4, 5, 6, 7);
}

constexpr int K1_T = 768;
constexpr int A_CH = 528;
constexpr int A_IMG = 2 * 56 * A_CH;
constexpr int B_CHUNK = 6 * 64 * 64;
constexpr int K1_LDS = A_IMG + 2 * B_CHUNK;

__global__ __launch_bounds__(K1_T) void k1_ln_qkv(const u16* __restrict__ x, const u16* __restrict__ wqkv,
                                                  const u16* __restrict__ wr, char* __restrict__ ws) {
  __shared__ __attribute__((aligned(16))) char smem[K1_LDS];
  char* Aimg = smem;
  char* Bbuf = smem + A_IMG;
  const int tid = threadIdx.x, lane = tid & 63, wave = tid >> 6;
  const int ng = blockIdx.x & 7, mb = blockIdx.x >> 3;
  const int row0 = mb * 64;

  const u16* bsrc0;
  const u16* bsrc1;
  int pc0, pc1;
  {
    int p = tid;
    int sl = p >> 8, k = (p >> 2) & 63;
    pc0 = p & 3;
    bsrc0 = wqkv + (size_t)k * N3 + 28 * (6 * ng + sl) + 8 * pc0;
    p = tid + 768;
    sl = p >> 8; k = (p >> 2) & 63;
    pc1 = p & 3;
    bsrc1 = wqkv + (size_t)k * N3 + 28 * (6 * ng + sl) + 8 * pc1;
  }
  auto load_piece = [&](const u16* src, int pc) -> u32x4 {
    u32x4 r;
    if (pc == 3) {
      u32x2 t = *(const u32x2*)src;
      r[0] = t[0]; r[1] = t[1]; r[2] = 0; r[3] = 0;
    } else {
      r = *(const u32x4*)src;
    }
    return r;
  };
  u32x4 breg0 = load_piece(bsrc0, pc0);
  u32x4 breg1 = load_piece(bsrc1, pc1);

  if (wave < 8) {
    const int rl = wave * 8 + (lane >> 3);
    const int i = lane & 7;
    const u16* xr = x + (size_t)(row0 + rl) * DM + i * 8;
    u32x4 v[7];
#pragma unroll
    for (int j = 0; j < 7; ++j) v[j] = *(const u32x4*)(xr + j * 64);
    float s = 0.f;
#pragma unroll
    for (int j = 0; j < 7; ++j) {
#pragma unroll
      for (int w = 0; w < 4; ++w) s = dot2bf(v[j][w], ONES_BF2, s);
    }
    s += __shfl_xor(s, 1); s += __shfl_xor(s, 2); s += __shfl_xor(s, 4);
    const float mean = s * (1.0f / 448.0f);
    float dd[56];
    float q = 0.f;
#pragma unroll
    for (int j = 0; j < 7; ++j) {
#pragma unroll
      for (int w = 0; w < 4; ++w) {
        float lo = bf_lo(v[j][w]) - mean, hi = bf_hi(v[j][w]) - mean;
        dd[j * 8 + 2 * w] = lo; dd[j * 8 + 2 * w + 1] = hi;
        q = fmaf(lo, lo, q); q = fmaf(hi, hi, q);
      }
    }
    q += __shfl_xor(q, 1); q += __shfl_xor(q, 2); q += __shfl_xor(q, 4);
    const float rstd = 1.0f / sqrtf(q * (1.0f / 448.0f) + LN_EPS);
    const int tt = rl >> 5, r = rl & 31;
    float rl_acc = 0.f;
#pragma unroll
    for (int j = 0; j < 7; ++j) {
      u32x4 pk;
#pragma unroll
      for (int w = 0; w < 4; ++w) pk[w] = pack_bf16(dd[j * 8 + 2 * w] * rstd, dd[j * 8 + 2 * w + 1] * rstd);
      const int c = i + 8 * j;
      *(u32x4*)(Aimg + (tt * 56 + c) * A_CH + r * 16) = pk;
      if (ng == 0) {
        u32x4 wv = *(const u32x4*)(wr + c * 8);
#pragma unroll
        for (int w = 0; w < 4; ++w) rl_acc = dot2bf(pk[w], wv[w], rl_acc);
      }
    }
    if (ng == 0) {
      rl_acc += __shfl_xor(rl_acc, 1); rl_acc += __shfl_xor(rl_acc, 2); rl_acc += __shfl_xor(rl_acc, 4);
      if (i == 0) {
        ((float*)(ws + OFF_R))[row0 + rl] = rl_acc;
        ((float2*)(ws + OFF_ST))[row0 + rl] = make_float2(mean, rstd);
      }
    }
  }

  const int tt = wave & 1, sl = wave >> 1;
  const int sig = ng * 6 + sl;
  const int typ = sig >> 4, head = sig & 15;
  const int r32 = lane & 31, h = lane >> 5;
  const int i16 = lane & 15, qq = i16 >> 2, pp = i16 & 3, cb = (lane >> 4) & 1;
  const char* a_base = Aimg + (tt * 56 + h) * A_CH + r32 * 16;
  const int b_off = sl * (64 * 64) + (8 * h + qq) * 64 + (16 * cb + 4 * pp) * 2;
  f32x16 acc = {};
  for (int kc = 0; kc < 7; ++kc) {
    char* bb = Bbuf + (kc & 1) * B_CHUNK;
    *(u32x4*)(bb + tid * 16) = breg0;
    *(u32x4*)(bb + (tid + 768) * 16) = breg1;
    __syncthreads();
    if (kc + 1 < 7) {
      breg0 = load_piece(bsrc0 + (size_t)(kc + 1) * 64 * N3, pc0);
      breg1 = load_piece(bsrc1 + (size_t)(kc + 1) * 64 * N3, pc1);
    }
#pragma unroll
    for (int s = 0; s < 4; ++s) {
      bf16x8 a = *(const bf16x8*)(a_base + (2 * (4 * kc + s)) * A_CH);
      bf16x8 b = tr_frag(bb + b_off + s * 16 * 64);
      if (typ == 2) acc = MFMA32(a, b, acc);
      else acc = MFMA32(b, a, acc);
    }
  }

  const int gtile = mb * 2 + tt;
  if (typ < 2) {
    char* dst = ws + (typ == 0 ? OFF_Q : OFF_K) + ((size_t)head * 64 + gtile) * TILE_B;
    const float sc = (typ == 0) ? QSCALE : 1.0f;
#pragma unroll
    for (int g = 0; g < 4; ++g) {
      u32x2 o;
      o[0] = pack_bf16(acc[4 * g] * sc, acc[4 * g + 1] * sc);
      o[1] = pack_bf16(acc[4 * g + 2] * sc, acc[4 * g + 3] * sc);
      if (g == 3 && h == 1) { o[0] = 0; o[1] = 0; }
      *(u32x2*)(dst + (g * 32 + r32) * 16 + 8 * h) = o;
    }
  } else {
    char* dst = ws + OFF_V + ((size_t)head * 64 + gtile) * TILE_B;
#pragma unroll
    for (int s = 0; s < 2; ++s) {
      u32x4 o;
#pragma unroll
      for (int w = 0; w < 4; ++w) o[w] = pack_bf16(acc[8 * s + 2 * w], acc[8 * s + 2 * w + 1]);
      if (r32 >= 28) {
        unsigned c = (r32 == 28) ? ONES_BF2 : 0u;
        o[0] = c; o[1] = c; o[2] = c; o[3] = c;
      }
      *(u32x4*)(dst + ((s * 2 + h) * 32 + r32) * 16) = o;
    }
  }
}

constexpr int K2_T = 512;
constexpr int ST_TILES = 4;
constexpr int ST_BYTES = ST_TILES * (int)TILE_B;
constexpr int NSTAGE = 64 / ST_TILES;
constexpr int K2_LDS = 2 * 2 * ST_BYTES;
constexpr float RESC_T = 20.0f;

__device__ __forceinline__ void moe_block(const u16* __restrict__ x, const u16* __restrict__ wg_,
                                          const u16* __restrict__ wu_, const u16* __restrict__ wd_,
                                          char* __restrict__ ws, char* smem) {
  const int tid = threadIdx.x, lane = tid & 63, wave = tid >> 6;
  const int g = blockIdx.x;
  float* sred = (float*)smem;
  float* xs = (float*)(smem + 8192);
  float* hs = (float*)(smem + 8192 + 2048);
  float* part = (float*)(smem + 16384);
  const float* rlog = (const float*)(ws + OFF_R);
  f32x4 rv = *(const f32x4*)(rlog + tid * 4);
  float bv = rv[0]; int bi = tid * 4;
#pragma unroll
  for (int j = 1; j < 4; ++j) if (rv[j] > bv) { bv = rv[j]; bi = tid * 4 + j; }
#pragma unroll
  for (int off = 1; off < 64; off <<= 1) {
    float ov = __shfl_xor(bv, off); int oi = __shfl_xor(bi, off);
    if (ov > bv || (ov == bv && oi < bi)) { bv = ov; bi = oi; }
  }
  if (lane == 0) { sred[wave] = bv; ((int*)sred)[8 + wave] = bi; }
  __syncthreads();
  bv = sred[0]; bi = ((int*)sred)[8];
#pragma unroll
  for (int w = 1; w < 8; ++w) {
    float ov = sred[w]; int oi = ((int*)sred)[8 + w];
    if (ov > bv || (ov == bv && oi < bi)) { bv = ov; bi = oi; }
  }
  const int idx = bi;
  const float2 st = ((const float2*)(ws + OFF_ST))[idx];
  if (tid < 448) {
    float xv = __uint_as_float((unsigned)x[(size_t)idx * DM + tid] << 16);
    xs[tid] = bfround((xv - st.x) * st.y);
  }
  if (g == 0 && tid == 0) {
    ((int*)(ws + OFF_MI))[0] = idx;
    ((float*)(ws + OFF_MI))[1] = 1.0f / (1.0f + __expf(-bv));
  }
  __syncthreads();
  const int f0 = g * MOE_FS;
  {
    const int cg = tid & 3, kg = tid >> 2;
    float ag[8], au[8];
#pragma unroll
    for (int j = 0; j < 8; ++j) { ag[j] = 0.f; au[j] = 0.f; }
#pragma unroll
    for (int it = 0; it < 4; ++it) {
      const int k = kg + 128 * it;
      if (k < DM) {
        u32x4 wgv = *(const u32x4*)(wg_ + (size_t)k * FF + f0 + cg * 8);
        u32x4 wuv = *(const u32x4*)(wu_ + (size_t)k * FF + f0 + cg * 8);
        const float xk = xs[k];
#pragma unroll
        for (int w = 0; w < 4; ++w) {
          ag[2 * w] = fmaf(xk, bf_lo(wgv[w]), ag[2 * w]);
          ag[2 * w + 1] = fmaf(xk, bf_hi(wgv[w]), ag[2 * w + 1]);
          au[2 * w] = fmaf(xk, bf_lo(wuv[w]), au[2 * w]);
          au[2 * w + 1] = fmaf(xk, bf_hi(wuv[w]), au[2 * w + 1]);
        }
      }
    }
#pragma unroll
    for (int off = 4; off < 64; off <<= 1) {
#pragma unroll
      for (int j = 0; j < 8; ++j) { ag[j] += __shfl_xor(ag[j], off); au[j] += __shfl_xor(au[j], off); }
    }
    if (lane < 4) {
#pragma unroll
      for (int j = 0; j < 8; ++j) {
        sred[wave * 64 + cg * 8 + j] = ag[j];
        sred[512 + wave * 64 + cg * 8 + j] = au[j];
      }
    }
  }
  __syncthreads();
  if (tid < 32) {
    float gs = 0.f, us = 0.f;
#pragma unroll
    for (int w = 0; w < 8; ++w) { gs += sred[w * 64 + tid]; us += sred[512 + w * 64 + tid]; }
    float sg = gs / (1.0f + __expf(-gs));
    hs[tid] = sg * us;
  }
  __syncthreads();
  if (tid < 448) {
    const int cg = tid % 56, fg = tid / 56;
    float ad[8];
#pragma unroll
    for (int j = 0; j < 8; ++j) ad[j] = 0.f;
#pragma unroll
    for (int it = 0; it < 4; ++it) {
      const int f = fg + 8 * it;
      u32x4 wv = *(const u32x4*)(wd_ + (size_t)(f0 + f) * DM + cg * 8);
      const float hk = hs[f];
#pragma unroll
      for (int w = 0; w < 4; ++w) {
        ad[2 * w] = fmaf(hk, bf_lo(wv[w]), ad[2 * w]);
        ad[2 * w + 1] = fmaf(hk, bf_hi(wv[w]), ad[2 * w + 1]);
      }
    }
#pragma unroll
    for (int j = 0; j < 8; ++j) part[fg * 448 + cg * 8 + j] = ad[j];
  }
  __syncthreads();
  if (tid < 448) {
    float s = 0.f;
#pragma unroll
    for (int f = 0; f < 8; ++f) s += part[f * 448 + tid];
    ((float*)(ws + OFF_MP))[g * 448 + tid] = s;
  }
}

__global__ __launch_bounds__(K2_T) void k2_attn(const u16* __restrict__ x, const u16* __restrict__ wg_,
                                                const u16* __restrict__ wu_, const u16* __restrict__ wd_,
                                                char* __restrict__ ws) {
  __shared__ __attribute__((aligned(16))) char smem[K2_LDS];
  if (blockIdx.x < MOE_G) {
    moe_block(x, wg_, wu_, wd_, ws, smem);
    return;
  }
  const int tid = threadIdx.x, lane = tid & 63, wave = tid >> 6;
  const int b = blockIdx.x - MOE_G;
  const int xcd = b & 7, jj = b >> 3;
  const int head = 2 * xcd + (jj & 1), qb = jj >> 1;
  const int qs = wave & 3, kg = wave >> 2;
  const int r32 = lane & 31, h = lane >> 5;

  const char* Kg = ws + OFF_K + (size_t)head * 64 * TILE_B;
  const char* Vg = ws + OFF_V + (size_t)head * 64 * TILE_B;
  const char* qt = ws + OFF_Q + ((size_t)head * 64 + qb * 4 + qs) * TILE_B;
  const bf16x8 q0 = *(const bf16x8*)(qt + lane * 16);
  const bf16x8 q1 = *(const bf16x8*)(qt + 1024 + lane * 16);

  u32x4 kst = *(const u32x4*)(Kg + tid * 16);
  u32x4 vst = *(const u32x4*)(Vg + tid * 16);

  f32x16 oacc = {};
  f32x16 negm = {};
  bool first = true;

  for (int st = 0; st < NSTAGE; ++st) {
    char* kb = smem + (st & 1) * (2 * ST_BYTES);
    char* vb = kb + ST_BYTES;
    *(u32x4*)(kb + tid * 16) = kst;
    *(u32x4*)(vb + tid * 16) = vst;
    __syncthreads();
    if (st + 1 < NSTAGE) {
      kst = *(const u32x4*)(Kg + (size_t)(st + 1) * ST_BYTES + tid * 16);
      vst = *(const u32x4*)(Vg + (size_t)(st + 1) * ST_BYTES + tid * 16);
    }
#pragma unroll
    for (int ti = 0; ti < ST_TILES / 2; ++ti) {
      const int t = 2 * ti + kg;
      const char* kp = kb + t * TILE_B + lane * 16;
      const char* vp = vb + t * TILE_B + lane * 16;
      bf16x8 k0 = *(const bf16x8*)(kp), k1 = *(const bf16x8*)(kp + 1024);
      bf16x8 v0 = *(const bf16x8*)(vp), v1 = *(const bf16x8*)(vp + 1024);
      f32x16 sacc = MFMA32(k0, q0, negm);
      sacc = MFMA32(k1, q1, sacc);
      float tmax = sacc[0];
#pragma unroll
      for (int i = 1; i < 16; ++i) tmax = fmaxf(tmax, sacc[i]);
      if (first || __any(tmax > RESC_T)) {
        auto sw = __builtin_amdgcn_permlane32_swap(__float_as_uint(tmax), __float_as_uint(tmax), false, false);
        float tm = fmaxf(__uint_as_float(sw[0]), __uint_as_float(sw[1]));
        float delta = first ? tm : fmaxf(tm, 0.f);
        float sc = first ? 0.f : __builtin_amdgcn_exp2f(-delta);
#pragma unroll
        for (int i = 0; i < 16; ++i) { oacc[i] *= sc; sacc[i] -= delta; negm[i] -= delta; }
        first = false;
      }
      unsigned pk[8];
#pragma unroll
      for (int i = 0; i < 8; ++i)
        pk[i] = pack_bf16(__builtin_amdgcn_exp2f(sacc[2 * i]), __builtin_amdgcn_exp2f(sacc[2 * i + 1]));
      bf16x8 p0 = __builtin_bit_cast(bf16x8, (u32x4){pk[0], pk[1], pk[2], pk[3]});
      bf16x8 p1 = __builtin_bit_cast(bf16x8, (u32x4){pk[4], pk[5], pk[6], pk[7]});
      oacc = MFMA32(v0, p0, oacc);
      oacc = MFMA32(v1, p1, oacc);
    }
  }
  __syncthreads();
  float* cbuf = (float*)smem;
  if (kg == 1) {
    float* p = cbuf + qs * 17 * 64 + lane;
    p[0] = -negm[0];
#pragma unroll
    for (int i = 0; i < 16; ++i) p[(i + 1) * 64] = oacc[i];
  }
  __syncthreads();
  if (kg == 0) {
    const float* p = cbuf + qs * 17 * 64 + lane;
    const float m0 = -negm[0], m1 = p[0];
    const float m = fmaxf(m0, m1);
    const float s0 = __builtin_amdgcn_exp2f(m0 - m), s1 = __builtin_amdgcn_exp2f(m1 - m);
#pragma unroll
    for (int i = 0; i < 16; ++i) oacc[i] = oacc[i] * s0 + p[(i + 1) * 64] * s1;
    float l = __shfl(oacc[12], r32 + 32);
    float inv = 1.0f / l;
    const int T = qb * 4 + qs;
    char* ob = ws + OFF_O + (size_t)T * 56 * 512 + r32 * 16;
#pragma unroll
    for (int g = 0; g < 4; ++g) {
      const int d0 = 8 * g + 4 * h;
      if (d0 < DH) {
        const int D0 = DH * head + d0;
        u32x2 o;
        o[0] = pack_bf16(oacc[4 * g] * inv, oacc[4 * g + 1] * inv);
        o[1] = pack_bf16(oacc[4 * g + 2] * inv, oacc[4 * g + 3] * inv);
        *(u32x2*)(ob + (D0 >> 3) * 512 + (D0 & 7) * 2) = o;
      }
    }
  }
}

constexpr int K3_T = 256;
constexpr int K3_LDS = 2 * 448 * 64;

__global__ __launch_bounds__(K3_T) void k3_out(const u16* __restrict__ x, const u16* __restrict__ wo,
                                               const char* __restrict__ ws, u16* __restrict__ out) {
  __shared__ __attribute__((aligned(16))) char smem[K3_LDS];
  const int tid = threadIdx.x, lane = tid & 63, wave = tid >> 6;
  const int cbk = blockIdx.x % 7, mb = blockIdx.x / 7;
  const int tt = wave & 1, ct = wave >> 1;
  const int r32 = lane & 31, h = lane >> 5;
  const int i16 = lane & 15, qq = i16 >> 2, pp = i16 & 3, cb = (lane >> 4) & 1;
  const int ncol0 = cbk * 64;
#pragma unroll
  for (int i = 0; i < 14; ++i) {
    const int p = tid + 256 * i;
    const int pan = (p >= 1792) ? 1 : 0;
    const int k = ((p >> 2) - pan * 448);
    const int pc = p & 3;
    u32x4 v = *(const u32x4*)(wo + (size_t)k * DM + ncol0 + pan * 32 + pc * 8);
    *(u32x4*)(smem + p * 16) = v;
  }
  const int T = mb * 2 + tt;
  const char* ab = ws + OFF_O + (size_t)T * 56 * 512 + lane * 16;
  bf16x8 af[28];
#pragma unroll
  for (int s = 0; s < 28; ++s) af[s] = *(const bf16x8*)(ab + s * 1024);
  __syncthreads();
  const char* bb = smem + ct * (448 * 64) + (8 * h + qq) * 64 + (16 * cb + 4 * pp) * 2;
  f32x16 acc = {};
#pragma unroll
  for (int s = 0; s < 28; ++s) {
    bf16x8 b = tr_frag(bb + s * 16 * 64);
    acc = MFMA32(b, af[s], acc);
  }
  const int token = T * 32 + r32;
  const float2 st = ((const float2*)(ws + OFF_ST))[token];
  const int idx = ((const int*)(ws + OFF_MI))[0];
  const float sig = ((const float*)(ws + OFF_MI))[1];
  const bool sel = (token == idx);
#pragma unroll
  for (int g = 0; g < 4; ++g) {
    const int n0 = ncol0 + ct * 32 + 8 * g + 4 * h;
    u32x2 xv = *(const u32x2*)(x + (size_t)token * DM + n0);
    float xf[4] = {bf_lo(xv[0]), bf_hi(xv[0]), bf_lo(xv[1]), bf_hi(xv[1])};
    float o[4];
#pragma unroll
    for (int j = 0; j < 4; ++j) {
      float z = bfround((xf[j] - st.x) * st.y);
      if (sel) {
        float y = 0.f;
        const float* mp = (const float*)(ws + OFF_MP) + n0 + j;
        for (int gg = 0; gg < MOE_G; ++gg) y += mp[gg * 448];
        z += y * sig;
      }
      float gate = z / (1.0f + __expf(-z));
      o[j] = xf[j] + acc[4 * g + j] * gate;
    }
    u32x2 ov;
    ov[0] = pack_bf16(o[0], o[1]);
    ov[1] = pack_bf16(o[2], o[3]);
    *(u32x2*)(out + (size_t)token * DM + n0) = ov;
  }
}

}

extern "C" void kernel_launch(void* const* d_in, const int* in_sizes, int n_in, void* d_out, int out_size,
                              void* d_ws, size_t ws_size, hipStream_t stream) {
  const u16* x = (const u16*)d_in[0];
  const u16* wqkv = (const u16*)d_in[1];
  const u16* wo = (const u16*)d_in[2];
  const u16* wr = (const u16*)d_in[4];
  const u16* wg = (const u16*)d_in[5];
  const u16* wu = (const u16*)d_in[6];
  const u16* wd = (const u16*)d_in[7];
  char* ws = (char*)d_ws;
  k1_ln_qkv<<<256, K1_T, 0, stream>>>(x, wqkv, wr, ws);
  k2_attn<<<256 + MOE_G, K2_T, 0, stream>>>(x, wg, wu, wd, ws);
  k3_out<<<224, K3_T, 0, stream>>>(x, wo, ws, (u16*)d_out);
}
```

```cpp
#include <hip/hip_runtime.h>
#include <hip/hip_bf16.h>
#include <stdint.h>

typedef short bf16x8 __attribute__((ext_vector_type(8)));
typedef short s16x4 __attribute__((ext_vector_type(4)));
typedef float f32x16 __attribute__((ext_vector_type(16)));
typedef float f32x4 __attribute__((ext_vector_type(4)));
typedef unsigned u32x4 __attribute__((ext_vector_type(4)));
typedef unsigned u32x2 __attribute__((ext_vector_type(2)));
typedef __bf16 bf2 __attribute__((ext_vector_type(2)));
typedef unsigned short u16;
#define LDSAS __attribute__((address_space(3)))

#define STAMP(i)
namespace {

constexpr int SEQ = 2048, DM = 448, NH = 16, DH = 28, FF = 1792, N3 = 1344;
constexpr float LN_EPS = 1e-5f;
constexpr float QSCALE = 0.18898223650461362f * 1.4426950408889634f;
constexpr unsigned ONES_BF2 = 0x3F803F80u;

constexpr size_t TILE_B = 2048;
constexpr size_t OFF_Q = 0;
constexpr size_t OFF_K = OFF_Q + (size_t)NH * 64 * TILE_B;
constexpr size_t OFF_V = OFF_K + (size_t)NH * 64 * TILE_B;
constexpr size_t OFF_O = OFF_V + (size_t)NH * 64 * TILE_B;
constexpr size_t OFF_R = OFF_O + (size_t)64 * 56 * 512;
constexpr size_t OFF_ST = OFF_R + 2048 * 4;
constexpr size_t OFF_MP = OFF_ST + 2048 * 8;
constexpr int MOE_G = 56;
constexpr int MOE_FS = FF / MOE_G;
constexpr size_t OFF_MI = OFF_MP + (size_t)MOE_G * 448 * 4;

__device__ __forceinline__ unsigned pack_bf16(float lo, float hi) {
  bf2 v = {(__bf16)lo, (__bf16)hi};
  return __builtin_bit_cast(unsigned, v);
}
__device__ __forceinline__ float bf_lo(unsigned u) { return __uint_as_float(u << 16); }
__device__ __forceinline__ float bf_hi(unsigned u) { return __uint_as_float(u & 0xffff0000u); }
__device__ __forceinline__ float dot2bf(unsigned a, unsigned b, float c) {
  return __builtin_amdgcn_fdot2_f32_bf16(__builtin_bit_cast(bf2, a), __builtin_bit_cast(bf2, b), c, false);
}
__device__ __forceinline__ float bfround(float x) {
  return (float)((__bf16)x);
}
#define MFMA32(a, b, c) __builtin_amdgcn_mfma_f32_32x32x16_bf16((a), (b), (c), 0, 0, 0)

__device__ __forceinline__ bf16x8 tr_frag2(const char* p, int off2) {
  s16x4 lo = __builtin_amdgcn_ds_read_tr16_b64_v4i16((LDSAS s16x4*)(p));
  s16x4 hi = __builtin_amdgcn_ds_read_tr16_b64_v4i16((LDSAS s16x4*)(p + off2));
  return __builtin_shufflevector(lo, hi, 0, 1, 2, 3, 4, 5, 6, 7);
}
__device__ __forceinline__ bf16x8 tr_frag(const char* p) {
  s16x4 lo = __builtin_amdgcn_ds_read_tr16_b64_v4i16((LDSAS s16x4*)(p));
  s16x4 hi = __builtin_amdgcn_ds_read_tr16_b64_v4i16((LDSAS s16x4*)(p + 256));
  return __builtin_shufflevector(lo, hi, 0, 1, 2, 3, 4, 5, 6, 7);
}

constexpr int K1_T = 768;
constexpr int A_CH = 528;
constexpr int A_IMG = 2 * 56 * A_CH;
constexpr int B_ROW = 448;
constexpr int B_CHUNK = 64 * B_ROW;
constexpr int K1_LDS = A_IMG + 2 * B_CHUNK;

__global__ __launch_bounds__(K1_T) void k1_ln_qkv(const u16* __restrict__ x, const u16* __restrict__ wqkv,
                                                  const u16* __restrict__ wr, char* __restrict__ ws) {
  __shared__ __attribute__((aligned(16))) char smem[K1_LDS];
  char* Aimg = smem;
  char* Bbuf = smem + A_IMG;
  const int tid = threadIdx.x, lane = tid & 63, wave = tid >> 6;
  const int xcd_ = blockIdx.x & 7, bi_ = blockIdx.x >> 3;
  const int ng = (xcd_ >> 2) * 4 + (bi_ & 3), mb = (xcd_ & 3) * 8 + (bi_ >> 2);
  const int row0 = mb * 64;
  constexpr int KID=0;
  STAMP(0);

  const int rl = (wave & 7) * 8 + (lane >> 3);
  const int i8 = lane & 7;
  u32x4 v[7];
  if (wave < 8) {
    const u16* xr = x + (size_t)(row0 + rl) * DM + i8 * 8;
#pragma unroll
    for (int j = 0; j < 7; ++j) v[j] = *(const u32x4*)(xr + j * 64);
  }

  const int kc0 = mb % 7;
  const bool second = __builtin_amdgcn_readfirstlane(wave) < 9;
  const int pk0 = tid / 21, pc0 = tid - pk0 * 21;
  const int p1 = second ? tid + 768 : tid;
  const int pk1 = p1 / 21, pc1 = p1 - pk1 * 21;
  u32x4 breg[14];
  {
    const u16* b0 = wqkv + (size_t)pk0 * N3 + ng * 168 + 8 * pc0;
    const u16* b1 = wqkv + (size_t)pk1 * N3 + ng * 168 + 8 * pc1;
#pragma unroll
    for (int i = 0; i < 7; ++i) {
      int kc = kc0 + i; if (kc >= 7) kc -= 7;
      const size_t off = (size_t)kc * 64 * N3;
      breg[2 * i] = *(const u32x4*)(b0 + off);
      breg[2 * i + 1] = *(const u32x4*)(b1 + off);
    }
  }

  if (wave < 8) {
    float s = 0.f;
#pragma unroll
    for (int j = 0; j < 7; ++j) {
#pragma unroll
      for (int w = 0; w < 4; ++w) s = dot2bf(v[j][w], ONES_BF2, s);
    }
    s += __shfl_xor(s, 1); s += __shfl_xor(s, 2); s += __shfl_xor(s, 4);
    const float mean = s * (1.0f / 448.0f);
    float q = 0.f;
#pragma unroll
    for (int j = 0; j < 7; ++j) {
#pragma unroll
      for (int w = 0; w < 4; ++w) {
        float lo = bf_lo(v[j][w]) - mean, hi = bf_hi(v[j][w]) - mean;
        q = fmaf(lo, lo, q); q = fmaf(hi, hi, q);
      }
    }
    q += __shfl_xor(q, 1); q += __shfl_xor(q, 2); q += __shfl_xor(q, 4);
    const float mbf = bfround(mean);
    const float den = bfround(sqrtf(bfround(bfround(q * (1.0f / 448.0f)) + LN_EPS)));
    const float inv = 1.0f / den;
    const int tt = rl >> 5, r = rl & 31;
    float rl_acc = 0.f;
#pragma unroll
    for (int j = 0; j < 7; ++j) {
      u32x4 pk;
#pragma unroll
      for (int w = 0; w < 4; ++w) {
        unsigned d = pack_bf16(bf_lo(v[j][w]) - mbf, bf_hi(v[j][w]) - mbf);
        pk[w] = pack_bf16(bf_lo(d) * inv, bf_hi(d) * inv);
      }
      const int c = i8 + 8 * j;
      *(u32x4*)(Aimg + (tt * 56 + c) * A_CH + r * 16) = pk;
      if (ng == 0) {
        u32x4 wv = *(const u32x4*)(wr + c * 8);
#pragma unroll
        for (int w = 0; w < 4; ++w) rl_acc = dot2bf(pk[w], wv[w], rl_acc);
      }
    }
    if (ng == 0) {
      rl_acc += __shfl_xor(rl_acc, 1); rl_acc += __shfl_xor(rl_acc, 2); rl_acc += __shfl_xor(rl_acc, 4);
      if (i8 == 0) {
        ((float*)(ws + OFF_R))[row0 + rl] = rl_acc;
        ((float2*)(ws + OFF_ST))[row0 + rl] = make_float2(mbf, inv);
      }
    }
  }
  STAMP(1);
  const int tt = wave & 1, sl = wave >> 1;
  const int sig = ng * 6 + sl;
  const int typ = sig >> 4, head = sig & 15;
  const int r32 = lane & 31, h = lane >> 5;
  const int i16 = lane & 15, qq = i16 >> 2, pp = i16 & 3, cb = (lane >> 4) & 1;
  const char* a_base = Aimg + (tt * 56 + h) * A_CH + r32 * 16;
  const int b_off = (8 * h + qq) * B_ROW + (28 * sl + 16 * cb + 4 * pp) * 2;
  const bool isV = __builtin_amdgcn_readfirstlane(typ) == 2;
  f32x16 acc = {};
#pragma unroll
  for (int i = 0; i < 7; ++i) {
    int kc = kc0 + i; if (kc >= 7) kc -= 7;
    char* bb = Bbuf + (i & 1) * B_CHUNK;
    *(u32x4*)(bb + pk0 * B_ROW + pc0 * 16) = breg[2 * i];
    if (second) *(u32x4*)(bb + pk1 * B_ROW + pc1 * 16) = breg[2 * i + 1];
    __syncthreads();
    const char* ap = a_base + (8 * kc) * A_CH;
    bf16x8 fa[4], fb[4];
#pragma unroll
    for (int s = 0; s < 4; ++s) {
      fa[s] = *(const bf16x8*)(ap + (2 * s) * A_CH);
      fb[s] = tr_frag2(bb + b_off + s * 16 * B_ROW, 4 * B_ROW);
    }
#pragma unroll
    for (int s = 0; s < 4; ++s) {
      bf16x8 X = isV ? fa[s] : fb[s];
      bf16x8 Y = isV ? fb[s] : fa[s];
      acc = MFMA32(X, Y, acc);
    }
  }
  STAMP(2);

  const int gtile = mb * 2 + tt;
  if (typ < 2) {
    char* dst = ws + (typ == 0 ? OFF_Q : OFF_K) + ((size_t)head * 64 + gtile) * TILE_B;
    const float sc = (typ == 0) ? QSCALE : 1.0f;
    const unsigned padv = (typ == 0) ? 0u : 0x00003F80u;
#pragma unroll
    for (int g = 0; g < 4; ++g) {
      u32x2 o;
      o[0] = pack_bf16(acc[4 * g] * sc, acc[4 * g + 1] * sc);
      o[1] = pack_bf16(acc[4 * g + 2] * sc, acc[4 * g + 3] * sc);
      if (g == 3 && h == 1) { o[0] = padv; o[1] = 0; }
      __builtin_nontemporal_store(o, (u32x2*)(dst + (g * 32 + r32) * 16 + 8 * h));
    }
  } else {
    char* dst = ws + OFF_V + ((size_t)head * 64 + gtile) * TILE_B;
#pragma unroll
    for (int s = 0; s < 2; ++s) {
      u32x4 o;
#pragma unroll
      for (int w = 0; w < 4; ++w) o[w] = pack_bf16(acc[8 * s + 2 * w], acc[8 * s + 2 * w + 1]);
      if (r32 >= 28) {
        unsigned c = (r32 == 28) ? ONES_BF2 : 0u;
        o[0] = c; o[1] = c; o[2] = c; o[3] = c;
      }
      __builtin_nontemporal_store(o, (u32x4*)(dst + ((s * 2 + h) * 32 + r32) * 16));
    }
  }
  STAMP(3);
}

constexpr int K2_T = 512;
constexpr int ST_TILES = 4;
constexpr int ST_BYTES = ST_TILES * (int)TILE_B;
constexpr int NSTAGE = 64 / ST_TILES;
constexpr int K2_LDS = 4 * 2 * ST_BYTES;
constexpr float RESC_T = 20.0f;

__device__ __forceinline__ void moe_block(const u16* __restrict__ x, const u16* __restrict__ wg_,
                                          const u16* __restrict__ wu_, const u16* __restrict__ wd_,
                                          char* __restrict__ ws, char* smem) {
  const int tid = threadIdx.x, lane = tid & 63, wave = tid >> 6;
  const int g = blockIdx.x;
  float* sred = (float*)smem;
  float* xs = (float*)(smem + 8192);
  float* hs = (float*)(smem + 8192 + 2048);
  float* part = (float*)(smem + 16384);
  const float* rlog = (const float*)(ws + OFF_R);
  f32x4 rv = *(const f32x4*)(rlog + tid * 4);
  float bv = rv[0]; int bi = tid * 4;
#pragma unroll
  for (int j = 1; j < 4; ++j) if (rv[j] > bv) { bv = rv[j]; bi = tid * 4 + j; }
#pragma unroll
  for (int off = 1; off < 64; off <<= 1) {
    float ov = __shfl_xor(bv, off); int oi = __shfl_xor(bi, off);
    if (ov > bv || (ov == bv && oi < bi)) { bv = ov; bi = oi; }
  }
  if (lane == 0) { sred[wave] = bv; ((int*)sred)[8 + wave] = bi; }
  __syncthreads();
  bv = sred[0]; bi = ((int*)sred)[8];
#pragma unroll
  for (int w = 1; w < 8; ++w) {
    float ov = sred[w]; int oi = ((int*)sred)[8 + w];
    if (ov > bv || (ov == bv && oi < bi)) { bv = ov; bi = oi; }
  }
  const int idx = bi;
  const float2 st = ((const float2*)(ws + OFF_ST))[idx];
  if (tid < 448) {
    float xv = __uint_as_float((unsigned)x[(size_t)idx * DM + tid] << 16);
    xs[tid] = bfround(bfround(xv - st.x) * st.y);
  }
  if (g == 0 && tid == 0) {
    ((int*)(ws + OFF_MI))[0] = idx;
    ((float*)(ws + OFF_MI))[1] = 1.0f / (1.0f + __expf(-bv));
  }
  __syncthreads();
  const int f0 = g * MOE_FS;
  {
    const int cg = tid & 3, kg = tid >> 2;
    float ag[8], au[8];
#pragma unroll
    for (int j = 0; j < 8; ++j) { ag[j] = 0.f; au[j] = 0.f; }
#pragma unroll
    for (int it = 0; it < 4; ++it) {
      const int k = kg + 128 * it;
      if (k < DM) {
        u32x4 wgv = *(const u32x4*)(wg_ + (size_t)k * FF + f0 + cg * 8);
        u32x4 wuv = *(const u32x4*)(wu_ + (size_t)k * FF + f0 + cg * 8);
        const float xk = xs[k];
#pragma unroll
        for (int w = 0; w < 4; ++w) {
          ag[2 * w] = fmaf(xk, bf_lo(wgv[w]), ag[2 * w]);
          ag[2 * w + 1] = fmaf(xk, bf_hi(wgv[w]), ag[2 * w + 1]);
          au[2 * w] = fmaf(xk, bf_lo(wuv[w]), au[2 * w]);
          au[2 * w + 1] = fmaf(xk, bf_hi(wuv[w]), au[2 * w + 1]);
        }
      }
    }
#pragma unroll
    for (int off = 4; off < 64; off <<= 1) {
#pragma unroll
      for (int j = 0; j < 8; ++j) { ag[j] += __shfl_xor(ag[j], off); au[j] += __shfl_xor(au[j], off); }
    }
    if (lane < 4) {
#pragma unroll
      for (int j = 0; j < 8; ++j) {
        sred[wave * 64 + cg * 8 + j] = ag[j];
        sred[512 + wave * 64 + cg * 8 + j] = au[j];
      }
    }
  }
  __syncthreads();
  if (tid < 32) {
    float gs = 0.f, us = 0.f;
#pragma unroll
    for (int w = 0; w < 8; ++w) { gs += sred[w * 64 + tid]; us += sred[512 + w * 64 + tid]; }
    float sg = gs / (1.0f + __expf(-gs));
    hs[tid] = sg * us;
  }
  __syncthreads();
  if (tid < 448) {
    const int cg = tid % 56, fg = tid / 56;
    float ad[8];
#pragma unroll
    for (int j = 0; j < 8; ++j) ad[j] = 0.f;
#pragma unroll
    for (int it = 0; it < 4; ++it) {
      const int f = fg + 8 * it;
      u32x4 wv = *(const u32x4*)(wd_ + (size_t)(f0 + f) * DM + cg * 8);
      const float hk = hs[f];
#pragma unroll
      for (int w = 0; w < 4; ++w) {
        ad[2 * w] = fmaf(hk, bf_lo(wv[w]), ad[2 * w]);
        ad[2 * w + 1] = fmaf(hk, bf_hi(wv[w]), ad[2 * w + 1]);
      }
    }
#pragma unroll
    for (int j = 0; j < 8; ++j) part[fg * 448 + cg * 8 + j] = ad[j];
  }
  __syncthreads();
  if (tid < 448) {
    float s = 0.f;
#pragma unroll
    for (int f = 0; f < 8; ++f) s += part[f * 448 + tid];
    ((float*)(ws + OFF_MP))[g * 448 + tid] = s;
  }
}

__device__ __forceinline__ unsigned bf16_ceil_bits(float x) {
  unsigned u = __float_as_uint(x);
  unsigned r = (x >= 0.f) ? ((u + 0xFFFFu) & 0xFFFF0000u) : (u & 0xFFFF0000u);
  return r;
}

constexpr int NBUF = 4;

__global__ __launch_bounds__(K2_T) void k2_attn(const u16* __restrict__ x, const u16* __restrict__ wg_,
                                                const u16* __restrict__ wu_, const u16* __restrict__ wd_,
                                                char* __restrict__ ws) {
  __shared__ __attribute__((aligned(16))) char smem[K2_LDS];
  if (blockIdx.x < MOE_G) {
    moe_block(x, wg_, wu_, wd_, ws, smem);
    return;
  }
  const int tid = threadIdx.x, lane = tid & 63, wave = tid >> 6;
  constexpr int KID=1;
  STAMP(0);
  const int b = blockIdx.x - MOE_G;
  const int xcd = b & 7, jj = b >> 3;
  const int head = 2 * xcd + (jj & 1), qb = jj >> 1;
  const int qs = wave & 3, kg = wave >> 2;
  const int r32 = lane & 31, h = lane >> 5;

  const char* Kg = ws + OFF_K + (size_t)head * 64 * TILE_B + tid * 16;
  const char* Vg = ws + OFF_V + (size_t)head * 64 * TILE_B + tid * 16;
  char* lbase = smem + wave * 1024;
  const int st0 = qb & (NSTAGE - 1);

  auto issue = [&](int it) {
    const int stg = (st0 + it) & (NSTAGE - 1);
    char* lb = lbase + (it % NBUF) * (2 * ST_BYTES);
    const char* kp = Kg + (size_t)stg * ST_BYTES;
    const char* vp = Vg + (size_t)stg * ST_BYTES;
    __builtin_amdgcn_global_load_lds((const unsigned*)(kp), (LDSAS unsigned*)(lb), 16, 0, 0);
    __builtin_amdgcn_global_load_lds((const unsigned*)(vp), (LDSAS unsigned*)(lb + ST_BYTES), 16, 0, 0);
  };
  issue(0); issue(1); issue(2);

  const char* qt = ws + OFF_Q + ((size_t)head * 64 + qb * 4 + qs) * TILE_B;
  const bf16x8 q0 = *(const bf16x8*)(qt + lane * 16);
  u32x4 q1u = *(const u32x4*)(qt + 1024 + lane * 16);

  f32x16 oacc = {};
  float mref = 0.f;
  bool first = true;
  const f32x16 zero = {};
  const char* lrd = smem + lane * 16 + kg * TILE_B;

#define LDK(K0, K1, BUFOFF, I) do { \
    const char* kp_ = lrd + (BUFOFF) + (2 * (I)) * TILE_B; \
    K0 = *(const bf16x8*)(kp_); K1 = *(const bf16x8*)(kp_ + 1024); } while (0)
#define QK(SD, K0, K1) do { \
    SD = MFMA32(K0, q0, zero); \
    SD = MFMA32(K1, __builtin_bit_cast(bf16x8, q1u), SD); } while (0)
#define LDV(VD0, VD1, BUFOFF, I) do { \
    const char* vp_ = lrd + (BUFOFF) + ST_BYTES + (2 * (I)) * TILE_B; \
    VD0 = *(const bf16x8*)(vp_); VD1 = *(const bf16x8*)(vp_ + 1024); } while (0)
#define PROCESS(SC, SN, V0, V1) do { \
    if (first || safe) { \
      float tmax_ = SC[0]; \
      _Pragma("unroll") for (int i_ = 1; i_ < 16; ++i_) tmax_ = fmaxf(tmax_, SC[i_]); \
      if (first || __any(tmax_ > RESC_T)) { \
        auto sw_ = __builtin_amdgcn_permlane32_swap(__float_as_uint(tmax_), __float_as_uint(tmax_), false, false); \
        float tm_ = fmaxf(__uint_as_float(sw_[0]), __uint_as_float(sw_[1])); \
        if (!first) tm_ = fmaxf(tm_, 0.f); \
        const float mnew_ = __uint_as_float(bf16_ceil_bits(mref + tm_)); \
        const float delta_ = mnew_ - mref; \
        const float sc_ = first ? 0.f : __builtin_amdgcn_exp2f(-delta_); \
        _Pragma("unroll") for (int i_ = 0; i_ < 16; ++i_) { oacc[i_] *= sc_; SC[i_] -= delta_; SN[i_] -= delta_; } \
        mref = mnew_; \
        if (h == 1) q1u[2] = __float_as_uint(-mref) >> 16; \
        first = false; \
      } \
    } \
    unsigned pk_[8]; \
    _Pragma("unroll") for (int i_ = 0; i_ < 8; ++i_) \
      pk_[i_] = pack_bf16(__builtin_amdgcn_exp2f(SC[2 * i_]), __builtin_amdgcn_exp2f(SC[2 * i_ + 1])); \
    bf16x8 p0_ = __builtin_bit_cast(bf16x8, (u32x4){pk_[0], pk_[1], pk_[2], pk_[3]}); \
    bf16x8 p1_ = __builtin_bit_cast(bf16x8, (u32x4){pk_[4], pk_[5], pk_[6], pk_[7]}); \
    oacc = MFMA32(V0, p0_, oacc); \
    oacc = MFMA32(V1, p1_, oacc); } while (0)

  f32x16 sA, sB;
  bf16x8 vA0, vA1, vB0, vB1, kA0, kA1, kB0, kB1;
  int* flag = (int*)(smem + K2_LDS - 16);
  for (int pass = 0; pass < 2; ++pass) {
    const bool safe = (pass == 1);
    if (pass == 1) {
      oacc = zero; mref = 0.f; first = true;
      q1u[2] = (h == 1) ? 0u : q1u[2];
      issue(0); issue(1); issue(2);
    }
    asm volatile("s_waitcnt vmcnt(4)" ::: "memory");
    __builtin_amdgcn_s_barrier();
    asm volatile("" ::: "memory");
    LDK(kA0, kA1, 0, 0);
    LDK(kB0, kB1, 0, 1);
    LDV(vA0, vA1, 0, 0);
    QK(sA, kA0, kA1);
    sB = zero;
    for (int it = 0; it < NSTAGE; ++it) {
      const int bo = (it & (NBUF - 1)) * (2 * ST_BYTES);
      const int bn = ((it + 1) & (NBUF - 1)) * (2 * ST_BYTES);
      const bool more = (it + 1 < NSTAGE);
      if (more) {
        if (it + 2 < NSTAGE) asm volatile("s_waitcnt vmcnt(2)" ::: "memory");
        else asm volatile("s_waitcnt vmcnt(0)" ::: "memory");
        __builtin_amdgcn_s_barrier();
        asm volatile("" ::: "memory");
        if (it + 3 < NSTAGE) issue(it + 3);
        LDK(kA0, kA1, bn, 0);
      }
      QK(sB, kB0, kB1);
      LDV(vB0, vB1, bo, 1);
      PROCESS(sA, sB, vA0, vA1);
      if (more) {
        LDK(kB0, kB1, bn, 1);
        QK(sA, kA0, kA1);
        LDV(vA0, vA1, bn, 0);
      }
      PROCESS(sB, sA, vB0, vB1);
    }
    if (safe) break;
    float amax = 0.f;
#pragma unroll
    for (int i = 0; i < 16; ++i) amax = fmaxf(amax, fabsf(oacc[i]));
    const bool bad = !(amax < 1.0e30f);
    __syncthreads();
    if (tid == 0) *flag = 0;
    __syncthreads();
    if (__any(bad) && lane == 0) atomicOr(flag, 1);
    __syncthreads();
    const int f = *flag;
    __syncthreads();
    if (f == 0) break;
  }
#undef QK
#undef LDK
#undef LDV
#undef PROCESS
  STAMP(1);
  __syncthreads();
  float* cbuf = (float*)smem;
  if (kg == 1) {
    float* p = cbuf + qs * 17 * 64 + lane;
    p[0] = mref;
#pragma unroll
    for (int i = 0; i < 16; ++i) p[(i + 1) * 64] = oacc[i];
  }
  __syncthreads();
  if (kg == 0) {
    const float* p = cbuf + qs * 17 * 64 + lane;
    const float m0 = mref, m1 = p[0];
    const float m = fmaxf(m0, m1);
    const float s0 = __builtin_amdgcn_exp2f(m0 - m), s1 = __builtin_amdgcn_exp2f(m1 - m);
#pragma unroll
    for (int i = 0; i < 16; ++i) oacc[i] = oacc[i] * s0 + p[(i + 1) * 64] * s1;
    float l = __shfl(oacc[12], r32 + 32);
    float inv = 1.0f / l;
    const int T = qb * 4 + qs;
    char* ob = ws + OFF_O + (size_t)T * 56 * 512 + r32 * 16;
#pragma unroll
    for (int g = 0; g < 4; ++g) {
      const int d0 = 8 * g + 4 * h;
      if (d0 < DH) {
        const int D0 = DH * head + d0;
        u32x2 o;
        o[0] = pack_bf16(oacc[4 * g] * inv, oacc[4 * g + 1] * inv);
        o[1] = pack_bf16(oacc[4 * g + 2] * inv, oacc[4 * g + 3] * inv);
        *(u32x2*)(ob + (D0 >> 3) * 512 + (D0 & 7) * 2) = o;
      }
    }
  }
  STAMP(2);
}

constexpr int K3_T = 256;
constexpr int K3_LDS = 2 * 448 * 64;

__global__ __launch_bounds__(K3_T) void k3_out(const u16* __restrict__ x, const u16* __restrict__ wo,
                                               const char* __restrict__ ws, u16* __restrict__ out) {
  __shared__ __attribute__((aligned(16))) char smem[K3_LDS];
  const int tid = threadIdx.x, lane = tid & 63, wave = tid >> 6;
  const int bi_ = blockIdx.x >> 3;
  const int cbk = bi_ % 7, mb = (blockIdx.x & 7) * 4 + bi_ / 7;
  const int tt = wave & 1, ct = wave >> 1;
  const int r32 = lane & 31, h = lane >> 5;
  const int i16 = lane & 15, qq = i16 >> 2, pp = i16 & 3, cb = (lane >> 4) & 1;
  const int ncol0 = cbk * 64;
  constexpr int KID=2;
  STAMP(0);
  const int T = mb * 2 + tt;
  const char* ab = ws + OFF_O + (size_t)T * 56 * 512 + lane * 16;
  bf16x8 af[28];
#pragma unroll
  for (int s = 0; s < 28; ++s) af[s] = *(const bf16x8*)(ab + s * 1024);
  const int token = T * 32 + r32;
  u32x2 xv[4];
#pragma unroll
  for (int g = 0; g < 4; ++g) xv[g] = *(const u32x2*)(x + (size_t)token * DM + ncol0 + ct * 32 + 8 * g + 4 * h);
  const float2 st = ((const float2*)(ws + OFF_ST))[token];
  const int idx = ((const int*)(ws + OFF_MI))[0];
  const float sig = ((const float*)(ws + OFF_MI))[1];
  __shared__ float ysh[4][64];
  const bool has_sel = (idx >= mb * 64) && (idx < mb * 64 + 64);
  if (has_sel) {
    const int c = tid & 63, gq = tid >> 6;
    const float* mp = (const float*)(ws + OFF_MP) + ncol0 + c;
    float a = 0.f;
#pragma unroll
    for (int gg = 0; gg < MOE_G / 4; ++gg) a += mp[(gq * (MOE_G / 4) + gg) * 448];
    ysh[gq][c] = a;
  }
#pragma unroll
  for (int i = 0; i < 14; ++i) {
    const int p = tid + 256 * i;
    const int pan = (p >= 1792) ? 1 : 0;
    const int k = ((p >> 2) - pan * 448);
    const int pc = p & 3;
    __builtin_amdgcn_global_load_lds((const unsigned*)(wo + (size_t)k * DM + ncol0 + pan * 32 + pc * 8),
                                     (LDSAS unsigned*)(smem + (wave * 64 + 256 * i) * 16), 16, 0, 0);
  }
  asm volatile("s_waitcnt vmcnt(0)" ::: "memory");
  __builtin_amdgcn_s_barrier();
  asm volatile("" ::: "memory");
  STAMP(1);
  const char* bb = smem + ct * (448 * 64) + (8 * h + qq) * 64 + (16 * cb + 4 * pp) * 2;
  f32x16 acc = {};
#pragma unroll
  for (int s = 0; s < 28; ++s) {
    bf16x8 b = tr_frag(bb + s * 16 * 64);
    acc = MFMA32(b, af[s], acc);
  }
  STAMP(2);
  const bool sel = (token == idx);
#pragma unroll
  for (int g = 0; g < 4; ++g) {
    const int n0 = ncol0 + ct * 32 + 8 * g + 4 * h;
    float xf[4] = {bf_lo(xv[g][0]), bf_hi(xv[g][0]), bf_lo(xv[g][1]), bf_hi(xv[g][1])};
    float o[4];
#pragma unroll
    for (int j = 0; j < 4; ++j) {
      float z = bfround(bfround(xf[j] - st.x) * st.y);
      if (sel) {
        const int cc = n0 + j - ncol0;
        z += (ysh[0][cc] + ysh[1][cc] + ysh[2][cc] + ysh[3][cc]) * sig;
      }
      float gate = z / (1.0f + __expf(-z));
      o[j] = xf[j] + acc[4 * g + j] * gate;
    }
    u32x2 ov;
    ov[0] = pack_bf16(o[0], o[1]);
    ov[1] = pack_bf16(o[2], o[3]);
    *(u32x2*)(out + (size_t)token * DM + n0) = ov;
  }
  STAMP(3);
}

}

extern "C" void kernel_launch(void* const* d_in, const int* in_sizes, int n_in, void* d_out, int out_size,
                              void* d_ws, size_t ws_size, hipStream_t stream) {
  const u16* x = (const u16*)d_in[0];
  const u16* wqkv = (const u16*)d_in[1];
  const u16* wo = (const u16*)d_in[2];
  const u16* wr = (const u16*)d_in[4];
  const u16* wg = (const u16*)d_in[5];
  const u16* wu = (const u16*)d_in[6];
  const u16* wd = (const u16*)d_in[7];
  char* ws = (char*)d_ws;
  k1_ln_qkv<<<256, K1_T, 0, stream>>>(x, wqkv, wr, ws);
  k2_attn<<<256 + MOE_G, K2_T, 0, stream>>>(x, wg, wu, wd, ws);
  k3_out<<<224, K3_T, 0, stream>>>(x, wo, ws, (u16*)d_out);
}
```

```cpp
#include <hip/hip_runtime.h>
#include <hip/hip_bf16.h>
#include <stdint.h>

typedef short bf16x8 __attribute__((ext_vector_type(8)));
typedef short s16x4 __attribute__((ext_vector_type(4)));
typedef float f32x16 __attribute__((ext_vector_type(16)));
typedef float f32x4 __attribute__((ext_vector_type(4)));
typedef unsigned u32x4 __attribute__((ext_vector_type(4)));
typedef unsigned u32x2 __attribute__((ext_vector_type(2)));
typedef __bf16 bf2 __attribute__((ext_vector_type(2)));
typedef unsigned short u16;
#define LDSAS __attribute__((address_space(3)))

#define STAMP(i)
namespace {

constexpr int SEQ = 2048, DM = 448, NH = 16, DH = 28, FF = 1792, N3 = 1344;
constexpr float LN_EPS = 1e-5f;
constexpr float QSCALE = 0.18898223650461362f * 1.4426950408889634f;
constexpr unsigned ONES_BF2 = 0x3F803F80u;

constexpr size_t TILE_B = 2048;
constexpr size_t OFF_Q = 0;
constexpr size_t OFF_K = OFF_Q + (size_t)NH * 64 * TILE_B;
constexpr size_t OFF_V = OFF_K + (size_t)NH * 64 * TILE_B;
constexpr size_t OFF_O = OFF_V + (size_t)NH * 64 * TILE_B;
constexpr size_t OFF_R = OFF_O + (size_t)64 * 56 * 512;
constexpr size_t OFF_ST = OFF_R + 2048 * 4;
constexpr size_t OFF_MP = OFF_ST + 2048 * 8;
#define MOE_GV 56
constexpr int MOE_G = MOE_GV;
constexpr int MOE_FS = FF / MOE_G;
constexpr size_t OFF_MI = OFF_MP + (size_t)MOE_G * 448 * 4;

__device__ __forceinline__ unsigned pack_bf16(float lo, float hi) {
  bf2 v = {(__bf16)lo, (__bf16)hi};
  return __builtin_bit_cast(unsigned, v);
}
__device__ __forceinline__ float bf_lo(unsigned u) { return __uint_as_float(u << 16); }
__device__ __forceinline__ float bf_hi(unsigned u) { return __uint_as_float(u & 0xffff0000u); }
__device__ __forceinline__ float dot2bf(unsigned a, unsigned b, float c) {
  return __builtin_amdgcn_fdot2_f32_bf16(__builtin_bit_cast(bf2, a), __builtin_bit_cast(bf2, b), c, false);
}
__device__ __forceinline__ float bfround(float x) {
  return (float)((__bf16)x);
}
#define MFMA32(a, b, c) __builtin_amdgcn_mfma_f32_32x32x16_bf16((a), (b), (c), 0, 0, 0)

__device__ __forceinline__ bf16x8 tr_frag2(const char* p, int off2) {
  s16x4 lo = __builtin_amdgcn_ds_read_tr16_b64_v4i16((LDSAS s16x4*)(p));
  s16x4 hi = __builtin_amdgcn_ds_read_tr16_b64_v4i16((LDSAS s16x4*)(p + off2));
  return __builtin_shufflevector(lo, hi, 0, 1, 2, 3, 4, 5, 6, 7);
}
__device__ __forceinline__ bf16x8 tr_frag(const char* p) {
  s16x4 lo = __builtin_amdgcn_ds_read_tr16_b64_v4i16((LDSAS s16x4*)(p));
  s16x4 hi = __builtin_amdgcn_ds_read_tr16_b64_v4i16((LDSAS s16x4*)(p + 256));
  return __builtin_shufflevector(lo, hi, 0, 1, 2, 3, 4, 5, 6, 7);
}

constexpr int K1_T = 768;
constexpr int A_CH = 528;
constexpr int A_IMG = 2 * 56 * A_CH;
constexpr int B_ROW = 448;
constexpr int B_CHUNK = 64 * B_ROW;
constexpr int K1_LDS = A_IMG + 2 * B_CHUNK;

__global__ __launch_bounds__(K1_T) void k1_ln_qkv(const u16* __restrict__ x, const u16* __restrict__ wqkv,
                                                  const u16* __restrict__ wr, char* __restrict__ ws) {
  __shared__ __attribute__((aligned(16))) char smem[K1_LDS];
  char* Aimg = smem;
  char* Bbuf = smem + A_IMG;
  const int tid = threadIdx.x, lane = tid & 63, wave = tid >> 6;
  const int xcd_ = blockIdx.x & 7, bi_ = blockIdx.x >> 3;
  const int ng = (xcd_ >> 2) * 4 + (bi_ & 3), mb = (xcd_ & 3) * 8 + (bi_ >> 2);
  const int row0 = mb * 64;
  constexpr int KID=0;
  STAMP(0);

  const int rl = (wave & 7) * 8 + (lane >> 3);
  const int i8 = lane & 7;
  u32x4 v[7];
  if (wave < 8) {
    const u16* xr = x + (size_t)(row0 + rl) * DM + i8 * 8;
#pragma unroll
    for (int j = 0; j < 7; ++j) v[j] = *(const u32x4*)(xr + j * 64);
  }

  const int kc0 = mb % 7;
  const bool second = __builtin_amdgcn_readfirstlane(wave) < 9;
  const int pk0 = tid / 21, pc0 = tid - pk0 * 21;
  const int p1 = second ? tid + 768 : tid;
  const int pk1 = p1 / 21, pc1 = p1 - pk1 * 21;
  u32x4 breg[14];
  {
    const u16* b0 = wqkv + (size_t)pk0 * N3 + ng * 168 + 8 * pc0;
    const u16* b1 = wqkv + (size_t)pk1 * N3 + ng * 168 + 8 * pc1;
#pragma unroll
    for (int i = 0; i < 7; ++i) {
      int kc = kc0 + i; if (kc >= 7) kc -= 7;
      const size_t off = (size_t)kc * 64 * N3;
      breg[2 * i] = *(const u32x4*)(b0 + off);
      breg[2 * i + 1] = *(const u32x4*)(b1 + off);
    }
  }

  if (wave < 8) {
    float s = 0.f;
#pragma unroll
    for (int j = 0; j < 7; ++j) {
#pragma unroll
      for (int w = 0; w < 4; ++w) s = dot2bf(v[j][w], ONES_BF2, s);
    }
    s += __shfl_xor(s, 1); s += __shfl_xor(s, 2); s += __shfl_xor(s, 4);
    const float mean = s * (1.0f / 448.0f);
    float q = 0.f;
#pragma unroll
    for (int j = 0; j < 7; ++j) {
#pragma unroll
      for (int w = 0; w < 4; ++w) {
        float lo = bf_lo(v[j][w]) - mean, hi = bf_hi(v[j][w]) - mean;
        q = fmaf(lo, lo, q); q = fmaf(hi, hi, q);
      }
    }
    q += __shfl_xor(q, 1); q += __shfl_xor(q, 2); q += __shfl_xor(q, 4);
    const float mbf = bfround(mean);
    const float den = bfround(sqrtf(bfround(bfround(q * (1.0f / 448.0f)) + LN_EPS)));
    const float inv = 1.0f / den;
    const int tt = rl >> 5, r = rl & 31;
    float rl_acc = 0.f;
#pragma unroll
    for (int j = 0; j < 7; ++j) {
      u32x4 pk;
#pragma unroll
      for (int w = 0; w < 4; ++w) {
        unsigned d = pack_bf16(bf_lo(v[j][w]) - mbf, bf_hi(v[j][w]) - mbf);
        pk[w] = pack_bf16(bf_lo(d) * inv, bf_hi(d) * inv);
      }
      const int c = i8 + 8 * j;
      *(u32x4*)(Aimg + (tt * 56 + c) * A_CH + r * 16) = pk;
      if (ng == 0) {
        u32x4 wv = *(const u32x4*)(wr + c * 8);
#pragma unroll
        for (int w = 0; w < 4; ++w) rl_acc = dot2bf(pk[w], wv[w], rl_acc);
      }
    }
    if (ng == 0) {
      rl_acc += __shfl_xor(rl_acc, 1); rl_acc += __shfl_xor(rl_acc, 2); rl_acc += __shfl_xor(rl_acc, 4);
      if (i8 == 0) {
        ((float*)(ws + OFF_R))[row0 + rl] = rl_acc;
        ((float2*)(ws + OFF_ST))[row0 + rl] = make_float2(mbf, inv);
      }
    }
  }
  STAMP(1);
  const int tt = wave & 1, sl = wave >> 1;
  const int sig = ng * 6 + sl;
  const int typ = sig >> 4, head = sig & 15;
  const int r32 = lane & 31, h = lane >> 5;
  const int i16 = lane & 15, qq = i16 >> 2, pp = i16 & 3, cb = (lane >> 4) & 1;
  const char* a_base = Aimg + (tt * 56 + h) * A_CH + r32 * 16;
  const int b_off = (8 * h + qq) * B_ROW + (28 * sl + 16 * cb + 4 * pp) * 2;
  const bool isV = __builtin_amdgcn_readfirstlane(typ) == 2;
  f32x16 acc = {};
#pragma unroll
  for (int i = 0; i < 7; ++i) {
    int kc = kc0 + i; if (kc >= 7) kc -= 7;
    char* bb = Bbuf + (i & 1) * B_CHUNK;
    *(u32x4*)(bb + pk0 * B_ROW + pc0 * 16) = breg[2 * i];
    if (second) *(u32x4*)(bb + pk1 * B_ROW + pc1 * 16) = breg[2 * i + 1];
    __syncthreads();
    const char* ap = a_base + (8 * kc) * A_CH;
    bf16x8 fa[4], fb[4];
#pragma unroll
    for (int s = 0; s < 4; ++s) {
      fa[s] = *(const bf16x8*)(ap + (2 * s) * A_CH);
      fb[s] = tr_frag2(bb + b_off + s * 16 * B_ROW, 4 * B_ROW);
    }
#pragma unroll
    for (int s = 0; s < 4; ++s) {
      bf16x8 X = isV ? fa[s] : fb[s];
      bf16x8 Y = isV ? fb[s] : fa[s];
      acc = MFMA32(X, Y, acc);
    }
  }
  STAMP(2);

  const int gtile = mb * 2 + tt;
  if (typ < 2) {
    char* dst = ws + (typ == 0 ? OFF_Q : OFF_K) + ((size_t)head * 64 + gtile) * TILE_B;
    const float sc = (typ == 0) ? QSCALE : 1.0f;
    const unsigned padv = (typ == 0) ? 0u : 0x00003F80u;
#pragma unroll
    for (int g = 0; g < 4; ++g) {
      u32x2 o;
      o[0] = pack_bf16(acc[4 * g] * sc, acc[4 * g + 1] * sc);
      o[1] = pack_bf16(acc[4 * g + 2] * sc, acc[4 * g + 3] * sc);
      if (g == 3 && h == 1) { o[0] = padv; o[1] = 0; }
      __builtin_nontemporal_store(o, (u32x2*)(dst + (g * 32 + r32) * 16 + 8 * h));
    }
  } else {
    char* dst = ws + OFF_V + ((size_t)head * 64 + gtile) * TILE_B;
#pragma unroll
    for (int s = 0; s < 2; ++s) {
      u32x4 o;
#pragma unroll
      for (int w = 0; w < 4; ++w) o[w] = pack_bf16(acc[8 * s + 2 * w], acc[8 * s + 2 * w + 1]);
      if (r32 >= 28) {
        unsigned c = (r32 == 28) ? ONES_BF2 : 0u;
        o[0] = c; o[1] = c; o[2] = c; o[3] = c;
      }
      __builtin_nontemporal_store(o, (u32x4*)(dst + ((s * 2 + h) * 32 + r32) * 16));
    }
  }
  STAMP(3);
}

constexpr int K2_T = 512;
constexpr int ST_TILES = 4;
constexpr int ST_BYTES = ST_TILES * (int)TILE_B;
constexpr int NSTAGE = 64 / ST_TILES;
constexpr int K2_LDS = 4 * 2 * ST_BYTES;
constexpr float RESC_T = 20.0f;

__device__ __forceinline__ void moe_block(const u16* __restrict__ x, const u16* __restrict__ wg_,
                                          const u16* __restrict__ wu_, const u16* __restrict__ wd_,
                                          char* __restrict__ ws, char* smem) {
  const int tid = threadIdx.x, lane = tid & 63, wave = tid >> 6;
  const int g = ((blockIdx.x - 256) & 7) * (MOE_G / 8) + ((blockIdx.x - 256) >> 3);
  float* sred = (float*)smem;
  float* xs = (float*)(smem + 8192);
  float* hs = (float*)(smem + 8192 + 2048);
  float* part = (float*)(smem + 16384);
  const float* rlog = (const float*)(ws + OFF_R);
  f32x4 rv = *(const f32x4*)(rlog + tid * 4);
  float bv = rv[0]; int bi = tid * 4;
#pragma unroll
  for (int j = 1; j < 4; ++j) if (rv[j] > bv) { bv = rv[j]; bi = tid * 4 + j; }
#pragma unroll
  for (int off = 1; off < 64; off <<= 1) {
    float ov = __shfl_xor(bv, off); int oi = __shfl_xor(bi, off);
    if (ov > bv || (ov == bv && oi < bi)) { bv = ov; bi = oi; }
  }
  if (lane == 0) { sred[wave] = bv; ((int*)sred)[8 + wave] = bi; }
  __syncthreads();
  bv = sred[0]; bi = ((int*)sred)[8];
#pragma unroll
  for (int w = 1; w < 8; ++w) {
    float ov = sred[w]; int oi = ((int*)sred)[8 + w];
    if (ov > bv || (ov == bv && oi < bi)) { bv = ov; bi = oi; }
  }
  const int idx = bi;
  const float2 st = ((const float2*)(ws + OFF_ST))[idx];
  if (tid < 448) {
    float xv = __uint_as_float((unsigned)x[(size_t)idx * DM + tid] << 16);
    xs[tid] = bfround(bfround(xv - st.x) * st.y);
  }
  if (g == 0 && tid == 0) {
    ((int*)(ws + OFF_MI))[0] = idx;
    ((float*)(ws + OFF_MI))[1] = 1.0f / (1.0f + __expf(-bv));
  }
  __syncthreads();
  constexpr int FS = MOE_FS, CG = FS / 8, KGN = 512 / CG, KIT = (DM + KGN - 1) / KGN;
  const int f0 = g * FS;
  {
    const int cg = tid % CG, kg = tid / CG;
    float ag[8], au[8];
#pragma unroll
    for (int j = 0; j < 8; ++j) { ag[j] = 0.f; au[j] = 0.f; }
#pragma unroll
    for (int it = 0; it < KIT; ++it) {
      const int k = kg + KGN * it;
      if (k < DM) {
        u32x4 wgv = *(const u32x4*)(wg_ + (size_t)k * FF + f0 + cg * 8);
        u32x4 wuv = *(const u32x4*)(wu_ + (size_t)k * FF + f0 + cg * 8);
        const float xk = xs[k];
#pragma unroll
        for (int w = 0; w < 4; ++w) {
          ag[2 * w] = fmaf(xk, bf_lo(wgv[w]), ag[2 * w]);
          ag[2 * w + 1] = fmaf(xk, bf_hi(wgv[w]), ag[2 * w + 1]);
          au[2 * w] = fmaf(xk, bf_lo(wuv[w]), au[2 * w]);
          au[2 * w + 1] = fmaf(xk, bf_hi(wuv[w]), au[2 * w + 1]);
        }
      }
    }
#pragma unroll
    for (int off = CG; off < 64; off <<= 1) {
#pragma unroll
      for (int j = 0; j < 8; ++j) { ag[j] += __shfl_xor(ag[j], off); au[j] += __shfl_xor(au[j], off); }
    }
    if (lane < CG) {
#pragma unroll
      for (int j = 0; j < 8; ++j) {
        sred[wave * 64 + cg * 8 + j] = ag[j];
        sred[512 + wave * 64 + cg * 8 + j] = au[j];
      }
    }
  }
  __syncthreads();
  if (tid < FS) {
    float gs = 0.f, us = 0.f;
#pragma unroll
    for (int w = 0; w < 8; ++w) { gs += sred[w * 64 + tid]; us += sred[512 + w * 64 + tid]; }
    float sg = gs / (1.0f + __expf(-gs));
    hs[tid] = sg * us;
  }
  __syncthreads();
  if (tid < 448) {
    const int cg = tid % 56, fg = tid / 56;
    float ad[8];
#pragma unroll
    for (int j = 0; j < 8; ++j) ad[j] = 0.f;
#pragma unroll
    for (int it = 0; it < FS / 8; ++it) {
      const int f = fg + 8 * it;
      u32x4 wv = *(const u32x4*)(wd_ + (size_t)(f0 + f) * DM + cg * 8);
      const float hk = hs[f];
#pragma unroll
      for (int w = 0; w < 4; ++w) {
        ad[2 * w] = fmaf(hk, bf_lo(wv[w]), ad[2 * w]);
        ad[2 * w + 1] = fmaf(hk, bf_hi(wv[w]), ad[2 * w + 1]);
      }
    }
#pragma unroll
    for (int j = 0; j < 8; ++j) part[fg * 448 + cg * 8 + j] = ad[j];
  }
  __syncthreads();
  if (tid < 448) {
    float s = 0.f;
#pragma unroll
    for (int f = 0; f < 8; ++f) s += part[f * 448 + tid];
    ((float*)(ws + OFF_MP))[g * 448 + tid] = s;
  }
}

__device__ __forceinline__ unsigned bf16_ceil_bits(float x) {
  unsigned u = __float_as_uint(x);
  unsigned r = (x >= 0.f) ? ((u + 0xFFFFu) & 0xFFFF0000u) : (u & 0xFFFF0000u);
  return r;
}

constexpr int NBUF = 4;

__global__ __launch_bounds__(K2_T) void k2_attn(const u16* __restrict__ x, const u16* __restrict__ wg_,
                                                const u16* __restrict__ wu_, const u16* __restrict__ wd_,
                                                char* __restrict__ ws) {
  __shared__ __attribute__((aligned(16))) char smem[K2_LDS];
  if (blockIdx.x >= 256) {
    moe_block(x, wg_, wu_, wd_, ws, smem);
    return;
  }
  const int tid = threadIdx.x, lane = tid & 63, wave = tid >> 6;
  constexpr int KID=1;
  STAMP(0);
  const int b = blockIdx.x;
  const int xcd = b & 7, jj = b >> 3;
  const int head = 2 * xcd + (jj & 1), qb = jj >> 1;
  const int qs = wave & 3, kg = wave >> 2;
  const int r32 = lane & 31, h = lane >> 5;

  const char* Kg = ws + OFF_K + (size_t)head * 64 * TILE_B + tid * 16;
  const char* Vg = ws + OFF_V + (size_t)head * 64 * TILE_B + tid * 16;
  char* lbase = smem + wave * 1024;
  const int st0 = qb & (NSTAGE - 1);

  auto issue = [&](int it) {
    const int stg = (st0 + it) & (NSTAGE - 1);
    char* lb = lbase + (it % NBUF) * (2 * ST_BYTES);
    const char* kp = Kg + (size_t)stg * ST_BYTES;
    const char* vp = Vg + (size_t)stg * ST_BYTES;
    __builtin_amdgcn_global_load_lds((const unsigned*)(kp), (LDSAS unsigned*)(lb), 16, 0, 0);
    __builtin_amdgcn_global_load_lds((const unsigned*)(vp), (LDSAS unsigned*)(lb + ST_BYTES), 16, 0, 0);
  };
  issue(0); issue(1); issue(2);

  const char* qt = ws + OFF_Q + ((size_t)head * 64 + qb * 4 + qs) * TILE_B;
  const bf16x8 q0 = *(const bf16x8*)(qt + lane * 16);
  u32x4 q1u = *(const u32x4*)(qt + 1024 + lane * 16);

  f32x16 oacc = {};
  float mref = 0.f;
  bool first = true;
  const f32x16 zero = {};
  const char* lrd = smem + lane * 16 + kg * TILE_B;

#define LDK(K0, K1, BUFOFF, I) do { \
    const char* kp_ = lrd + (BUFOFF) + (2 * (I)) * TILE_B; \
    K0 = *(const bf16x8*)(kp_); K1 = *(const bf16x8*)(kp_ + 1024); } while (0)
#define QK(SD, K0, K1) do { \
    SD = MFMA32(K0, q0, zero); \
    SD = MFMA32(K1, __builtin_bit_cast(bf16x8, q1u), SD); } while (0)
#define LDV(VD0, VD1, BUFOFF, I) do { \
    const char* vp_ = lrd + (BUFOFF) + ST_BYTES + (2 * (I)) * TILE_B; \
    VD0 = *(const bf16x8*)(vp_); VD1 = *(const bf16x8*)(vp_ + 1024); } while (0)
#define PROCESS(SC, SN, V0, V1) do { \
    if (first || safe) { \
      float tmax_ = SC[0]; \
      _Pragma("unroll") for (int i_ = 1; i_ < 16; ++i_) tmax_ = fmaxf(tmax_, SC[i_]); \
      if (first || __any(tmax_ > RESC_T)) { \
        auto sw_ = __builtin_amdgcn_permlane32_swap(__float_as_uint(tmax_), __float_as_uint(tmax_), false, false); \
        float tm_ = fmaxf(__uint_as_float(sw_[0]), __uint_as_float(sw_[1])); \
        if (!first) tm_ = fmaxf(tm_, 0.f); \
        const float mnew_ = __uint_as_float(bf16_ceil_bits(mref + tm_)); \
        const float delta_ = mnew_ - mref; \
        const float sc_ = first ? 0.f : __builtin_amdgcn_exp2f(-delta_); \
        _Pragma("unroll") for (int i_ = 0; i_ < 16; ++i_) { oacc[i_] *= sc_; SC[i_] -= delta_; SN[i_] -= delta_; } \
        mref = mnew_; \
        if (h == 1) q1u[2] = __float_as_uint(-mref) >> 16; \
        first = false; \
      } \
    } \
    unsigned pk_[8]; \
    _Pragma("unroll") for (int i_ = 0; i_ < 8; ++i_) \
      pk_[i_] = pack_bf16(__builtin_amdgcn_exp2f(SC[2 * i_]), __builtin_amdgcn_exp2f(SC[2 * i_ + 1])); \
    bf16x8 p0_ = __builtin_bit_cast(bf16x8, (u32x4){pk_[0], pk_[1], pk_[2], pk_[3]}); \
    bf16x8 p1_ = __builtin_bit_cast(bf16x8, (u32x4){pk_[4], pk_[5], pk_[6], pk_[7]}); \
    oacc = MFMA32(V0, p0_, oacc); \
    oacc = MFMA32(V1, p1_, oacc); } while (0)

  f32x16 sA, sB;
  bf16x8 vA0, vA1, vB0, vB1, kA0, kA1, kB0, kB1;
  for (int pass = 0; pass < 2; ++pass) {
    const bool safe = (pass == 1);
    if (pass == 1) {
      oacc = zero; mref = 0.f; first = true;
      q1u[2] = (h == 1) ? 0u : q1u[2];
      issue(0); issue(1); issue(2);
    }
    asm volatile("s_waitcnt vmcnt(4)" ::: "memory");
    __builtin_amdgcn_s_barrier();
    asm volatile("" ::: "memory");
    LDK(kA0, kA1, 0, 0);
    LDK(kB0, kB1, 0, 1);
    LDV(vA0, vA1, 0, 0);
    QK(sA, kA0, kA1);
    sB = zero;
    for (int it = 0; it < NSTAGE; ++it) {
      const int bo = (it & (NBUF - 1)) * (2 * ST_BYTES);
      const int bn = ((it + 1) & (NBUF - 1)) * (2 * ST_BYTES);
      const bool more = (it + 1 < NSTAGE);
      if (more) {
        if (it + 2 < NSTAGE) asm volatile("s_waitcnt vmcnt(2)" ::: "memory");
        else asm volatile("s_waitcnt vmcnt(0)" ::: "memory");
        __builtin_amdgcn_s_barrier();
        asm volatile("" ::: "memory");
        if (it + 3 < NSTAGE) issue(it + 3);
        LDK(kA0, kA1, bn, 0);
      }
      QK(sB, kB0, kB1);
      LDV(vB0, vB1, bo, 1);
      PROCESS(sA, sB, vA0, vA1);
      if (more) {
        LDK(kB0, kB1, bn, 1);
        QK(sA, kA0, kA1);
        LDV(vA0, vA1, bn, 0);
      }
      PROCESS(sB, sA, vB0, vB1);
    }
    if (safe) break;
    float amax = 0.f;
#pragma unroll
    for (int i = 0; i < 16; ++i) amax = fmaxf(amax, fabsf(oacc[i]));
    const int bad = !(amax < 1.0e30f);
    if (__syncthreads_or(bad) == 0) break;
  }
#undef QK
#undef LDK
#undef LDV
#undef PROCESS
  STAMP(1);
  __syncthreads();
  float* cbuf = (float*)smem;
  if (kg == 1) {
    float* p = cbuf + qs * 17 * 64 + lane;
    p[0] = mref;
#pragma unroll
    for (int i = 0; i < 16; ++i) p[(i + 1) * 64] = oacc[i];
  }
  __syncthreads();
  if (kg == 0) {
    const float* p = cbuf + qs * 17 * 64 + lane;
    const float m0 = mref, m1 = p[0];
    const float m = fmaxf(m0, m1);
    const float s0 = __builtin_amdgcn_exp2f(m0 - m), s1 = __builtin_amdgcn_exp2f(m1 - m);
#pragma unroll
    for (int i = 0; i < 16; ++i) oacc[i] = oacc[i] * s0 + p[(i + 1) * 64] * s1;
    float l = __shfl(oacc[12], r32 + 32);
    float inv = 1.0f / l;
    const int T = qb * 4 + qs;
    char* ob = ws + OFF_O + (size_t)T * 56 * 512 + r32 * 16;
#pragma unroll
    for (int g = 0; g < 4; ++g) {
      const int d0 = 8 * g + 4 * h;
      if (d0 < DH) {
        const int D0 = DH * head + d0;
        u32x2 o;
        o[0] = pack_bf16(oacc[4 * g] * inv, oacc[4 * g + 1] * inv);
        o[1] = pack_bf16(oacc[4 * g + 2] * inv, oacc[4 * g + 3] * inv);
        __builtin_nontemporal_store(o, (u32x2*)(ob + (D0 >> 3) * 512 + (D0 & 7) * 2));
      }
    }
  }
  STAMP(2);
}

constexpr int K3_T = 256;
constexpr int K3_LDS = 2 * 448 * 64;

__global__ __launch_bounds__(K3_T) void k3_out(const u16* __restrict__ x, const u16* __restrict__ wo,
                                               const char* __restrict__ ws, u16* __restrict__ out) {
  __shared__ __attribute__((aligned(16))) char smem[K3_LDS];
  const int tid = threadIdx.x, lane = tid & 63, wave = tid >> 6;
  const int bi_ = blockIdx.x >> 3;
  const int cbk = bi_ % 7, mb = (blockIdx.x & 7) * 4 + bi_ / 7;
  const int tt = wave & 1, ct = wave >> 1;
  const int r32 = lane & 31, h = lane >> 5;
  const int i16 = lane & 15, qq = i16 >> 2, pp = i16 & 3, cb = (lane >> 4) & 1;
  const int ncol0 = cbk * 64;
  constexpr int KID=2;
  STAMP(0);
  const int T = mb * 2 + tt;
  const char* ab = ws + OFF_O + (size_t)T * 56 * 512 + lane * 16;
  bf16x8 af[28];
#pragma unroll
  for (int s = 0; s < 28; ++s) af[s] = *(const bf16x8*)(ab + s * 1024);
  const int token = T * 32 + r32;
  u32x2 xv[4];
#pragma unroll
  for (int g = 0; g < 4; ++g) xv[g] = *(const u32x2*)(x + (size_t)token * DM + ncol0 + ct * 32 + 8 * g + 4 * h);
  const float2 st = ((const float2*)(ws + OFF_ST))[token];
  const int idx = ((const int*)(ws + OFF_MI))[0];
  const float sig = ((const float*)(ws + OFF_MI))[1];
  __shared__ float ysh[4][64];
  const bool has_sel = (idx >= mb * 64) && (idx < mb * 64 + 64);
  if (has_sel) {
    const int c = tid & 63, gq = tid >> 6;
    const float* mp = (const float*)(ws + OFF_MP) + ncol0 + c;
    float a = 0.f;
#pragma unroll
    for (int gg = 0; gg < MOE_G / 4; ++gg) a += mp[(gq * (MOE_G / 4) + gg) * 448];
    ysh[gq][c] = a;
  }
#pragma unroll
  for (int i = 0; i < 14; ++i) {
    const int p = tid + 256 * i;
    const int pan = (p >= 1792) ? 1 : 0;
    const int k = ((p >> 2) - pan * 448);
    const int pc = p & 3;
    __builtin_amdgcn_global_load_lds((const unsigned*)(wo + (size_t)k * DM + ncol0 + pan * 32 + pc * 8),
                                     (LDSAS unsigned*)(smem + (wave * 64 + 256 * i) * 16), 16, 0, 0);
  }
  asm volatile("s_waitcnt vmcnt(0)" ::: "memory");
  __builtin_amdgcn_s_barrier();
  asm volatile("" ::: "memory");
  STAMP(1);
  const char* bb = smem + ct * (448 * 64) + (8 * h + qq) * 64 + (16 * cb + 4 * pp) * 2;
  f32x16 acc = {};
#pragma unroll
  for (int s = 0; s < 28; ++s) {
    bf16x8 b = tr_frag(bb + s * 16 * 64);
    acc = MFMA32(b, af[s], acc);
  }
  STAMP(2);
  const bool sel = (token == idx);
#pragma unroll
  for (int g = 0; g < 4; ++g) {
    const int n0 = ncol0 + ct * 32 + 8 * g + 4 * h;
    float xf[4] = {bf_lo(xv[g][0]), bf_hi(xv[g][0]), bf_lo(xv[g][1]), bf_hi(xv[g][1])};
    float o[4];
#pragma unroll
    for (int j = 0; j < 4; ++j) {
      float z = bfround(bfround(xf[j] - st.x) * st.y);
      if (sel) {
        const int cc = n0 + j - ncol0;
        z += (ysh[0][cc] + ysh[1][cc] + ysh[2][cc] + ysh[3][cc]) * sig;
      }
      float gate = z / (1.0f + __expf(-z));
      o[j] = xf[j] + acc[4 * g + j] * gate;
    }
    u32x2 ov;
    ov[0] = pack_bf16(o[0], o[1]);
    ov[1] = pack_bf16(o[2], o[3]);
    *(u32x2*)(out + (size_t)token * DM + n0) = ov;
  }
  STAMP(3);
}

}

extern "C" void kernel_launch(void* const* d_in, const int* in_sizes, int n_in, void* d_out, int out_size,
                              void* d_ws, size_t ws_size, hipStream_t stream) {
  const u16* x = (const u16*)d_in[0];
  const u16* wqkv = (const u16*)d_in[1];
  const u16* wo = (const u16*)d_in[2];
  const u16* wr = (const u16*)d_in[4];
  const u16* wg = (const u16*)d_in[5];
  const u16* wu = (const u16*)d_in[6];
  const u16* wd = (const u16*)d_in[7];
  char* ws = (char*)d_ws;
  k1_ln_qkv<<<256, K1_T, 0, stream>>>(x, wqkv, wr, ws);
  k2_attn<<<256 + MOE_G, K2_T, 0, stream>>>(x, wg, wu, wd, ws);
  k3_out<<<224, K3_T, 0, stream>>>(x, wo, ws, (u16*)d_out);
}
```
